```python
import torch

def _softmax(x, axis=-1):
    return torch.softmax(x, dim=axis)
def _gelu(x):
    return torch.nn.functional.gelu(x, approximate="tanh")
def _silu(x):
    return torch.nn.functional.silu(x)
def _sigmoid(x):
    return torch.sigmoid(x)
def _layernorm(x, axis=-1, eps=1e-5):
    m = x.mean(dim=axis, keepdim=True)
    v = x.var(dim=axis, keepdim=True, unbiased=False)
    return (x - m) / torch.sqrt(v + eps)
def _scatter_add(tgt, idx, src, axis=0):
    return tgt.clone().index_add_(axis, idx, src)
def _segment_sum(src, seg, n):
    out = torch.zeros((n,) + src.shape[1:], dtype=src.dtype)
    return out.index_add_(0, seg, src)
def _pad(x, pw):
    flat = tuple(v for pair in reversed(pw) for v in pair)
    return torch.nn.functional.pad(x, flat)

kernel_name = 'concat_heads__sliding_local_dilated__sliding_block__prompt'

def setup_inputs():

    _w = (lambda a: (lambda t: t.to(torch.bfloat16) if t.is_floating_point() else t)(torch.as_tensor(a)))
    def _A_setup():
        B, S, D = 1, 2048, 640
        return {
            "x": _w((lambda *s: torch.randn(*s, dtype=torch.bfloat16) * 0.2)(B, S, D)),
            "wqkv": _w((lambda *s: torch.randn(*s, dtype=torch.bfloat16) * 0.2)(D, 3 * D)),
            "wo": _w((lambda *s: torch.randn(*s, dtype=torch.bfloat16) * 0.05)(D, D)),
        }
    _A_in = _A_setup()

    def _B_setup():
        B, S, D = 1, 2048, 640
        return {
            "x": _w((lambda *s: torch.randn(*s, dtype=torch.bfloat16) * 0.2)(B, S, D)),
            "wqkv": _w((lambda *s: torch.randn(*s, dtype=torch.bfloat16) * 0.2)(D, 3 * D)),
            "wo": _w((lambda *s: torch.randn(*s, dtype=torch.bfloat16) * 0.05)(D, D)),
        }
    _B_in = _B_setup()

    return {
        "A_x": _A_in["x"],
        "A_wqkv": _A_in["wqkv"],
        "A_wo": _A_in["wo"],
        "B_x": _B_in["x"],
        "B_wqkv": _B_in["wqkv"],
        "B_wo": _B_in["wo"],
    }

def reference(A_x, A_wqkv, A_wo, B_x, B_wqkv, B_wo):
    def _A_ref(x, wqkv, wo):
        B, S, D = x.shape
        H = 32; Dh = D // H
        W, dil = 8, 2
        qkv = (lambda x, *d: x.permute(*d))((x @ wqkv).reshape(B, S, 3, H, Dh), 2, 0, 3, 1, 4)
        q, k, v = qkv[0], qkv[1], qkv[2]
        scale = Dh ** -0.5
        pos = torch.arange(S)
        off = torch.arange(W) * dil
        idx = pos[:, None] - off[None, :]
        mask = idx >= 0
        idx_c = torch.where(mask, idx, 0).reshape(-1)
        kg = k[:, :, idx_c, :].reshape(B, H, S, W, Dh)
        vg = v[:, :, idx_c, :].reshape(B, H, S, W, Dh)
        logits = torch.einsum("bhsd,bhswd->bhsw", q, kg) * scale
        logits = torch.where(mask[None, None, :, :], logits, -1e9)
        attn = _softmax(logits, axis=-1)
        o = torch.einsum("bhsw,bhswd->bhsd", attn, vg)
        return (lambda x, *d: x.permute(*d))(o, 0, 2, 1, 3).reshape(B, S, D) @ wo

    def _B_ref(x, wqkv, wo):
        B, S, D = x.shape
        H = 32; Dh = D // H; W = 256
        qkv = (lambda x, *d: x.permute(*d))((x @ wqkv).reshape(B, S, 3, H, Dh), 2, 0, 3, 1, 4)
        q, k, v = qkv[0], qkv[1], qkv[2]
        scores = torch.einsum("bhqd,bhkd->bhqk", q, k) * (Dh ** -0.5)
        idx = torch.arange(S)
        mask = (idx[:, None] - idx[None, :] >= 0) & (idx[:, None] - idx[None, :] < W)
        scores = torch.where(mask[None, None], scores, -1e9)
        p = _softmax(scores, axis=-1)
        o = torch.einsum("bhqk,bhkd->bhqd", p, v)
        return (lambda x, *d: x.permute(*d))(o, 0, 2, 1, 3).reshape(B, S, D) @ wo

    a = _A_ref(A_x, A_wqkv, A_wo)
    b = _B_ref(B_x, B_wqkv, B_wo)
    return torch.concatenate([a, b], axis=-1)
```

```cpp
#include <hip/hip_runtime.h>
#include <hip/hip_bf16.h>
#include <stdint.h>

typedef __bf16 bf16x8 __attribute__((ext_vector_type(8)));
typedef __bf16 bf16x4 __attribute__((ext_vector_type(4)));
typedef __bf16 bf16x2 __attribute__((ext_vector_type(2)));
typedef short s16x4 __attribute__((ext_vector_type(4)));
typedef float f32x4 __attribute__((ext_vector_type(4)));
typedef float f32x2 __attribute__((ext_vector_type(2)));
typedef unsigned u32x4 __attribute__((ext_vector_type(4)));
typedef unsigned u32x2 __attribute__((ext_vector_type(2)));
typedef unsigned long long u64;
#define LAS __attribute__((address_space(3)))
#define GAS __attribute__((address_space(1)))
#define DI __device__ __forceinline__

constexpr int NWG = 256, NTHR = 512;
constexpr int LDS_BYTES = 163840;
constexpr int STAGE = 49152, WOFF = 16384;
constexpr int KT = 2560, VT = 2688;
constexpr int KB = 0, VB = 24 * KT, QB = VB + 24 * VT;
constexpr int WO_BYTES = 640 * 160;
constexpr int RED = WO_BYTES;
static_assert(QB + 8 * KT <= LDS_BYTES && RED + 3 * 20480 <= LDS_BYTES && 3 * STAGE <= LDS_BYTES, "lds");
constexpr size_t WS_EPOCH = 0;
constexpr size_t WS_KVF = 4096;
constexpr size_t WS_OF = WS_KVF + 256 * 128;
constexpr size_t WS_STAMP = WS_OF + 256 * 128;
constexpr size_t WS_KV = 1 << 20;
constexpr size_t KV_BYTES = 40960;
constexpr size_t WS_O = WS_KV + 256 * KV_BYTES;
constexpr size_t O_TILE = 20480;
constexpr int SPIN_CAP = 1 << 18;

#define STAMP(i) do {} while (0)

DI f32x4 mfma32(bf16x8 a, bf16x8 b, f32x4 c) { return __builtin_amdgcn_mfma_f32_16x16x32_bf16(a, b, c, 0, 0, 0); }
DI unsigned pk2(float a, float b) { bf16x2 v = {(__bf16)a, (__bf16)b}; return __builtin_bit_cast(unsigned, v); }
DI u32x2 pk4(f32x4 v) { u32x2 r = {pk2(v[0], v[1]), pk2(v[2], v[3])}; return r; }
DI bf16x8 cat(u32x2 lo, u32x2 hi) { u32x4 v = {lo[0], lo[1], hi[0], hi[1]}; return __builtin_bit_cast(bf16x8, v); }
DI u32x2 lds_r64(LAS char* p) { return *(LAS u32x2*)p; }
DI u32x2 lds_tr(LAS char* p) { return __builtin_bit_cast(u32x2, __builtin_amdgcn_ds_read_tr16_b64_v4i16((LAS s16x4*)p)); }
DI void glds1(const char* sbase, unsigned v0, unsigned lds) {
  unsigned keep;
  asm volatile("s_mov_b32 %0, m0\n\ts_mov_b32 m0, %3\n\ts_nop 0\n\tglobal_load_lds_dwordx4 %1, %2\n\ts_mov_b32 m0, %0"
               : "=&s"(keep) : "v"(v0), "s"(sbase), "s"(lds) : "memory");
}
DI void glds2(const char* sbase, unsigned v0, unsigned v1, unsigned lds) {
  unsigned keep;
  asm volatile("s_mov_b32 %0, m0\n\ts_mov_b32 m0, %4\n\ts_nop 0\n\tglobal_load_lds_dwordx4 %1, %3\n\t"
               "global_load_lds_dwordx4 %2, %3 offset:1024\n\ts_mov_b32 m0, %0"
               : "=&s"(keep) : "v"(v0), "v"(v1), "s"(sbase), "s"(lds) : "memory");
}
DI void glds4(const char* sbase, unsigned v0, unsigned v1, unsigned v2, unsigned v3, unsigned lds) {
  unsigned keep;
  asm volatile("s_mov_b32 %0, m0\n\ts_mov_b32 m0, %6\n\ts_nop 0\n\tglobal_load_lds_dwordx4 %1, %5\n\t"
               "global_load_lds_dwordx4 %2, %5 offset:1024\n\tglobal_load_lds_dwordx4 %3, %5 offset:2048\n\t"
               "global_load_lds_dwordx4 %4, %5 offset:3072\n\ts_mov_b32 m0, %0"
               : "=&s"(keep) : "v"(v0), "v"(v1), "v"(v2), "v"(v3), "s"(sbase), "s"(lds) : "memory");
}
#define SB() __builtin_amdgcn_sched_barrier(0)
DI u64 mix64(u64 x) { x += 0x9E3779B97F4A7C15ull; x = (x ^ (x >> 30)) * 0xBF58476D1CE4E5B9ull; x = (x ^ (x >> 27)) * 0x94D049BB133111EBull; return x ^ (x >> 31); }
#define WAIT_VM(n) asm volatile("s_waitcnt vmcnt(" #n ")" ::: "memory")
#define WAIT_LGKM0() do { asm volatile("" ::: "memory"); __builtin_amdgcn_s_waitcnt(0xC07F); asm volatile("" ::: "memory"); } while (0)
#define BAR() __builtin_amdgcn_s_barrier()
DI void bar_lds() { WAIT_LGKM0(); BAR(); }

DI void flag_set(char* ws, size_t off, u64 tag) { __hip_atomic_store((GAS u64*)(ws + off), tag, __ATOMIC_RELAXED, __HIP_MEMORY_SCOPE_AGENT); }
DI void flag_wait(char* ws, size_t off, bool active, u64 tag) {
  for (int it = 0; it < SPIN_CAP; ++it) {
    u64 v = tag;
    if (active) v = __hip_atomic_load((GAS u64*)(ws + off), __ATOMIC_RELAXED, __HIP_MEMORY_SCOPE_AGENT);
    if (__all(v == tag)) break;
    __builtin_amdgcn_s_sleep(1);
  }
  asm volatile("" ::: "memory");
}
DI float xmax(float m) {
  auto a = __builtin_amdgcn_permlane32_swap(__float_as_uint(m), __float_as_uint(m), false, false);
  m = fmaxf(__uint_as_float(a[0]), __uint_as_float(a[1]));
  auto b = __builtin_amdgcn_permlane16_swap(__float_as_uint(m), __float_as_uint(m), false, false);
  return fmaxf(__uint_as_float(b[0]), __uint_as_float(b[1]));
}

template <int NW, int MODE>
DI void attn_pair(LAS char* smem, int lane, int h, int rho0, int qt0, int tfirst,
                  u32x2 (&omain)[2], u32x2 (&otail)[2]) {
  constexpr int NT = NW + 1;
  const int r = lane & 15, g = lane >> 4, q = (lane >> 2) & 3, p = lane & 3;
  const float NEG = -__builtin_inff();
  const float CS = 0.22360679774997896f * 1.4426950408889634f;
  f32x4 mfirst, mlast, zero = {0.f, 0.f, 0.f, 0.f}, neg4 = {NEG, NEG, NEG, NEG};
#pragma unroll
  for (int e = 0; e < 4; ++e) {
    const int key = 4 * g + e;
    if (MODE == 0) {
      mfirst[e] = (key - r >= 2 && ((key - r) & 1) == 0) ? 0.f : NEG;
      mlast[e] = (r - key >= 0 && ((r - key) & 1) == 0) ? 0.f : NEG;
    } else {
      mfirst[e] = key > r ? 0.f : NEG;
      mlast[e] = key <= r ? 0.f : NEG;
    }
  }
  bf16x8 qf[2];
#pragma unroll
  for (int t = 0; t < 2; ++t) {
    LAS char* qp = smem + QB + (qt0 + t) * KT + 5 * h * 128 + r * 8;
    u32x2 lo = lds_r64(qp + g * 128), hi = lds_r64(qp + 512);
    if (g != 0) { hi[0] = 0; hi[1] = 0; }
    qf[t] = cat(lo, hi);
  }
  LAS char* kp = smem + KB + rho0 * KT + 5 * h * 128 + r * 8;
  f32x4 s[2][NW];
#pragma unroll
  for (int u = 0; u < NT; ++u) {
    bf16x8 kf = cat(lds_r64(kp + g * 128 + u * KT), lds_r64(kp + 512 + u * KT));
    const bool dead = (MODE != 1) && (tfirst + u < 0);
    if (u < NW) {
      f32x4 c = u == 0 ? mfirst : (u == NW - 1 ? mlast : zero);
      if (MODE != 1) c = dead ? neg4 : c;
      s[0][u] = mfma32(kf, qf[0], c);
    }
    if (u >= 1) {
      f32x4 c = u == 1 ? mfirst : (u == NW ? mlast : zero);
      if (MODE != 1) c = dead ? neg4 : c;
      s[1][u - 1] = mfma32(kf, qf[1], c);
    }
  }
  u32x2 pp[2][NT];
#pragma unroll
  for (int t = 0; t < 2; ++t) {
    float m = fmaxf(fmaxf(s[t][0][0], s[t][0][1]), fmaxf(s[t][0][2], s[t][0][3]));
#pragma unroll
    for (int u = 1; u < NW; ++u) {
      m = fmaxf(fmaxf(m, s[t][u][0]), s[t][u][1]);
      m = fmaxf(fmaxf(m, s[t][u][2]), s[t][u][3]);
    }
    m = xmax(m);
    const float mc = -m * CS;
#pragma unroll
    for (int u = 0; u < NW; ++u) {
      f32x4 e;
#pragma unroll
      for (int k = 0; k < 4; ++k) e[k] = __builtin_amdgcn_exp2f(__builtin_fmaf(s[t][u][k], CS, mc));
      pp[t][u + t] = pk4(e);
    }
    pp[t][t == 0 ? NW : 0] = u32x2{0u, 0u};
  }
  const int c0 = 5 * h + p, c1 = 5 * h + 4;
  LAS char* vlo = smem + VB + rho0 * VT + c0 * 128 + (((4 * g + q) ^ ((c0 & 2) << 2)) * 8);
  LAS char* vhi = smem + VB + rho0 * VT + (p == 0 ? c1 * 128 + (((4 * g + q) ^ ((c1 & 2) << 2)) * 8) : 2560);
  f32x4 olo[2] = {zero, zero}, ohi[2] = {zero, zero};
  constexpr int NS = (NT + 1) / 2;
#pragma unroll
  for (int st = 0; st < NS; ++st) {
    const int u0 = 2 * st, u1 = (2 * st + 1 < NT) ? 2 * st + 1 : 2 * st;
    bf16x8 va = cat(lds_tr(vlo + u0 * VT), lds_tr(vlo + u1 * VT));
    bf16x8 vb = cat(lds_tr(vhi + u0 * VT), lds_tr(vhi + u1 * VT));
#pragma unroll
    for (int t = 0; t < 2; ++t) {
      bf16x8 pf = cat(pp[t][u0], (2 * st + 1 < NT) ? pp[t][u1] : u32x2{0u, 0u});
      olo[t] = mfma32(va, pf, olo[t]);
      ohi[t] = mfma32(vb, pf, ohi[t]);
    }
  }
#pragma unroll
  for (int t = 0; t < 2; ++t) {
    auto a = __builtin_amdgcn_permlane32_swap(__float_as_uint(ohi[t][0]), __float_as_uint(ohi[t][0]), false, false);
    const float inv = __builtin_amdgcn_rcpf(__uint_as_float(a[1]));
    omain[t] = pk4(olo[t] * inv);
    otail[t] = pk4(ohi[t] * inv);
  }
}

template <int NW, int MODE>
DI void attn_wave(LAS char* smem, __amdgpu_buffer_rsrc_t orsrc, int obase, int lane, int w, int rt, int hg) {
  const int hp = w & 1, a = w >> 1;
  const int rho0 = 16 + 2 * a - (NW - 1);
  const int tfirst = 8 * rt - 16 + rho0;
  u32x2 m0[2], t0[2], m1[2], t1[2];
  attn_pair<NW, MODE>(smem, lane, 2 * hp, rho0, 2 * a, tfirst, m0, t0);
  attn_pair<NW, MODE>(smem, lane, 2 * hp + 1, rho0, 2 * a, tfirst, m1, t1);
  const int HP = 2 * hg + hp;
#pragma unroll
  for (int t = 0; t < 2; ++t) {
    const int tb = obase + (8 * rt + 2 * a + t) * (int)O_TILE;
    u32x4 vm = {m0[t][0], m0[t][1], m1[t][0], m1[t][1]}, vt = {t0[t][0], t0[t][1], t1[t][0], t1[t][1]};
    __builtin_amdgcn_raw_buffer_store_b128(vm, orsrc, HP * 1024 + lane * 16, tb, 16);
    if (lane < 16) __builtin_amdgcn_raw_buffer_store_b128(vt, orsrc, 16384 + HP * 256 + lane * 16, tb, 16);
  }
}

DI void load_wo(LAS char* smem, const char* wo, int hg, int lane, int w) {
#pragma unroll 1
  for (int pc = w; pc < 100; pc += 8) {
    const int idx = pc * 64 + lane, row = (idx * 6554) >> 16, ch = idx - row * 10;
    glds1(wo + hg * 160, row * 1280 + ch * 16, (unsigned)(size_t)smem + pc * 1024);
  }
}

DI void outproj64(LAS char* smem, __amdgpu_buffer_rsrc_t orsrc, int otile_off, __hip_bfloat16* outp, int call, int lane, int w) {
  const int r = lane & 15, g = lane >> 4, q = (lane >> 2) & 3, p = lane & 3;
  const int tt = w & 3, kp = w >> 2;
  u32x4 of[10];
  const int ob = otile_off + tt * (int)O_TILE;
#pragma unroll
  for (int s = 0; s < 8; ++s) of[s] = __builtin_amdgcn_raw_buffer_load_b128(orsrc, lane * 16 + s * 1024, ob + kp * 8192, 16);
#pragma unroll
  for (int s = 0; s < 2; ++s) of[8 + s] = __builtin_amdgcn_raw_buffer_load_b128(orsrc, lane * 16 + s * 1024, ob + 16384 + kp * 2048, 16);
  SB();
  LAS char* wm = smem + kp * 51200 + (4 * g + q) * 160 + p * 8;
  LAS char* wt = smem + kp * 51200 + (40 * g + 16 + q) * 160 + p * 8;
  f32x4 acc[5];
#pragma unroll
  for (int i = 0; i < 5; ++i) acc[i] = f32x4{0.f, 0.f, 0.f, 0.f};
#pragma unroll
  for (int s = 0; s < 10; ++s) {
    LAS char* b = s < 8 ? wm + s * 6400 : wt + (s - 8) * 25600;
#pragma unroll
    for (int i = 0; i < 5; ++i) {
      bf16x8 wf = cat(lds_tr(b + i * 32), lds_tr(b + 3200 + i * 32));
      acc[i] = mfma32(wf, __builtin_bit_cast(bf16x8, of[s]), acc[i]);
    }
  }
  LAS char* red = smem + RED + call * 20480 + tt * 5120 + lane * 16;
  if (kp == 1) {
#pragma unroll
    for (int i = 0; i < 5; ++i) *(LAS f32x4*)(red + i * 1024) = acc[i];
  }
  bar_lds();
  if (kp == 0) {
    char* o = (char*)outp + (size_t)(tt * 16 + r) * 2560 + g * 8;
#pragma unroll
    for (int i = 0; i < 5; ++i) {
      f32x4 v = acc[i] + *(LAS f32x4*)(red + i * 1024);
      *(GAS u32x2*)(o + i * 32) = pk4(v);
    }
  }
}

__global__ void __launch_bounds__(NTHR, 2)
fused_kernel(const char* __restrict__ xA, const char* __restrict__ wqA, const char* __restrict__ woA,
             const char* __restrict__ xB, const char* __restrict__ wqB, const char* __restrict__ woB,
             __hip_bfloat16* __restrict__ out, char* ws) {
  __shared__ __attribute__((aligned(16))) char smem_[LDS_BYTES];
  LAS char* smem = (LAS char*)smem_;
  const int tid = threadIdx.x, lane = tid & 63;
  const int w = __builtin_amdgcn_readfirstlane(tid >> 6);
  const int b = blockIdx.x, xcd = b & 7, idx = b >> 3;
  const int br = xcd & 1, rt = ((xcd >> 1) & 1) * 8 + (idx & 7), hg = (xcd >> 2) * 4 + (idx >> 3);
  const int wgid = (br * 16 + rt) * 8 + hg;
  const int r = lane & 15, g = lane >> 4, q = (lane >> 2) & 3, p = lane & 3;
  STAMP(0);

  const char* xsrc = (br ? xB : xA) + (size_t)rt * 128 * 1280;
  const char* wsrc = (br ? wqB : wqA) + hg * 160;
  unsigned xoff[2], woff[4];
#pragma unroll
  for (int i = 0; i < 2; ++i) {
    const int row = 16 * w + 8 * i + (lane >> 3);
    xoff[i] = row * 1280 + (((lane & 7) ^ ((row >> 1) & 7)) << 4) - i * 1024;
  }
#pragma unroll
  for (int i = 0; i < 4; ++i) {
    const int row = 8 * w + 2 * i + (lane >> 5), pc = lane & 31;
    int c = (pc >> 1) ^ ((row & 3) | (((row >> 3) & 1) << 2));
    c = c == 15 ? 14 : c;
    const int seg = (c * 13) >> 6, c5 = c - 5 * seg;
    woff[i] = row * 3840 + seg * 1280 + c5 * 32 + (pc & 1) * 16 - i * 1024;
  }
  const unsigned lds0 = (unsigned)(size_t)smem;
  auto issue = [&](int s, int buf) {
    glds2(xsrc + s * 128, xoff[0], xoff[1], lds0 + buf * STAGE + w * 2048);
    glds4(wsrc + (size_t)s * (64 * 3840), woff[0], woff[1], woff[2], woff[3], lds0 + buf * STAGE + WOFF + w * 4096);
  };
  issue(0, 0); issue(1, 1); issue(2, 2);
  u64 epoch;
  asm volatile("s_load_dwordx2 %0, %1, 0x0\n\ts_waitcnt lgkmcnt(0)" : "=s"(epoch) : "s"(ws) : "memory");
  const u64 tag = mix64(epoch);

  const int wt = w & 1, wc = w >> 1;
  const int hl = q | ((g & 1) << 2);
  int xa[2], wa[4];
#pragma unroll
  for (int ks = 0; ks < 2; ++ks) xa[ks] = (64 * wt + r) * 128 + (((4 * ks + g) ^ ((r >> 1) & 7)) << 4);
#pragma unroll
  for (int i = 0; i < 4; ++i) wa[i] = WOFF + (8 * g + q) * 512 + (((4 * wc + i) ^ hl) << 5) + p * 8;
  f32x4 acc[4][4];
#pragma unroll
  for (int i = 0; i < 4; ++i)
#pragma unroll
    for (int j = 0; j < 4; ++j) acc[i][j] = f32x4{0.f, 0.f, 0.f, 0.f};

  bf16x8 xf0[4], wf0[4], xf1[4], wf1[4];
  auto rdfrag = [&](int buf, int ks, bf16x8 (&xf)[4], bf16x8 (&wf)[4]) {
    LAS char* base = smem + buf * STAGE;
#pragma unroll
    for (int i = 0; i < 4; ++i) {
      wf[i] = cat(lds_tr(base + wa[i] + ks * 16384), lds_tr(base + wa[i] + ks * 16384 + 2048));
      xf[i] = *(LAS bf16x8*)(base + xa[ks] + i * 2048);
    }
  };
  auto domma = [&](bf16x8 (&xf)[4], bf16x8 (&wf)[4]) {
#pragma unroll
    for (int i = 0; i < 4; ++i)
#pragma unroll
      for (int j = 0; j < 4; ++j) acc[i][j] = mfma32(wf[i], xf[j], acc[i][j]);
  };
  WAIT_VM(12);
  BAR();
  rdfrag(0, 0, xf0, wf0);
  int buf = 0;
#pragma unroll 1
  for (int s = 0; s < 10; ++s) {
    rdfrag(buf, 1, xf1, wf1);
    SB();
    domma(xf0, wf0);
    SB();
    if (s < 8) { WAIT_VM(6); } else { WAIT_VM(0); }
    bar_lds();
    if (s < 7) issue(s + 3, buf);
    buf = buf == 2 ? 0 : buf + 1;
    if (s < 9) rdfrag(buf, 0, xf0, wf0);
    SB();
    domma(xf1, wf1);
    SB();
  }
  STAMP(1);

  const __amdgpu_buffer_rsrc_t kvrsrc = __builtin_amdgcn_make_buffer_rsrc(ws + WS_KV, 0, (int)(256 * KV_BYTES), 0x00020000);
  const __amdgpu_buffer_rsrc_t orsrc = __builtin_amdgcn_make_buffer_rsrc(ws + WS_O, 0, (int)(256 * O_TILE), 0x00020000);
  const int vlane = lane ^ ((lane & 32) >> 2);
#pragma unroll
  for (int i = 0; i < 4; ++i) {
    const int c = 4 * wc + i;
    if (c < 15) {
      const int seg = (c * 13) >> 6, c5 = c - 5 * seg;
      const int tstride = seg == 2 ? VT : KT;
      const int tb = seg == 0 ? QB + 4 * wt * KT : (seg == 1 ? KB + (16 + 4 * wt) * KT : VB + (16 + 4 * wt) * VT);
      LAS char* d = smem + tb + c5 * 512 + (seg == 2 ? vlane : lane) * 8;
      u32x2 v[4];
#pragma unroll
      for (int j = 0; j < 4; ++j) { v[j] = pk4(acc[i][j]); *(LAS u32x2*)(d + j * tstride) = v[j]; }
      if (seg != 0) {
        const int so = wgid * (int)KV_BYTES + (c - 5) * 4096 + wt * 2048;
        if (br) __builtin_amdgcn_raw_buffer_store_b128(u32x4{v[0][0], v[0][1], v[1][0], v[1][1]}, kvrsrc, lane * 16, so, 16);
        if (br || wt) __builtin_amdgcn_raw_buffer_store_b128(u32x4{v[2][0], v[2][1], v[3][0], v[3][1]}, kvrsrc, lane * 16, so + 1024, 16);
      }
    }
  }
  if (tid < 24) *(LAS u32x2*)(smem + VB + tid * VT + 2560) = u32x2{0x3F803F80u, 0x3F803F80u};
  WAIT_VM(0);
  bar_lds();
  if (tid == 0) flag_set(ws, WS_KVF + (size_t)wgid * 128, tag);
  STAMP(2);

  const int nnb = br ? (rt < 2 ? rt : 2) : (rt < 1 ? 0 : 1);
  if (w == 0) flag_wait(ws, WS_KVF + (size_t)(wgid - 8 * (lane + 1)) * 128, lane < nnb, tag);
  BAR();
  STAMP(3);
  if (br) {
    u32x4 v[10];
#pragma unroll
    for (int k = 0; k < 10; ++k) {
      const int nb = k / 5, pc = (k % 5) * 8 + w;
      const bool ok = rt - 2 + nb >= 0;
      v[k] = __builtin_amdgcn_raw_buffer_load_b128(kvrsrc, ok ? lane * 16 : 0x7FFF0000, ok ? (wgid - 8 * (2 - nb)) * (int)KV_BYTES + pc * 1024 : 0, 16);
    }
    SB();
#pragma unroll
    for (int k = 0; k < 10; ++k) {
      const int nb = k / 5, pc = (k % 5) * 8 + w, c = pc >> 2, pr = pc & 3;
      const bool isv = c >= 5;
      const int ts = isv ? VT : KT, tile = nb * 8 + pr * 2;
      LAS char* d = smem + (isv ? VB + (c - 5) * 512 + vlane * 8 : KB + c * 512 + lane * 8) + tile * ts;
      *(LAS u32x2*)d = u32x2{v[k][0], v[k][1]};
      *(LAS u32x2*)(d + ts) = u32x2{v[k][2], v[k][3]};
    }
  } else {
#pragma unroll
    for (int k = 0; k < 2; ++k) {
      const int c = k * 8 + w;
      if (c < 10) {
        const bool ok = rt >= 1;
        u32x4 v = __builtin_amdgcn_raw_buffer_load_b128(kvrsrc, ok ? lane * 16 : 0x7FFF0000, ok ? (wgid - 8) * (int)KV_BYTES + c * 4096 + 3072 : 0, 16);
        const bool isv = c >= 5;
        LAS char* d = smem + (isv ? VB + (c - 5) * 512 + vlane * 8 + 15 * VT : KB + c * 512 + lane * 8 + 15 * KT);
        *(LAS u32x2*)d = u32x2{v[2], v[3]};
      }
    }
  }
  bar_lds();
  STAMP(4);
  const int obase = br * 128 * (int)O_TILE;
  if (br == 0) attn_wave<2, 0>(smem, orsrc, obase, lane, w, rt, hg);
  else if (rt >= 2) attn_wave<17, 1>(smem, orsrc, obase, lane, w, rt, hg);
  else attn_wave<17, 2>(smem, orsrc, obase, lane, w, rt, hg);
  STAMP(5);
  WAIT_VM(0);
  bar_lds();
  if (tid == 0) flag_set(ws, WS_OF + (size_t)wgid * 128, tag);
  STAMP(6);

  if (br == 0) {
    load_wo(smem, woA, hg, lane, w);
    if (w == 0) flag_wait(ws, WS_OF + (size_t)(rt * 8 + lane) * 128, lane < 8, tag);
    WAIT_VM(0);
    BAR();
    STAMP(7);
    outproj64(smem, orsrc, (8 * rt) * (int)O_TILE, out + (size_t)(128 * rt) * 1280 + hg * 80, 0, lane, w);
    outproj64(smem, orsrc, (8 * rt + 4) * (int)O_TILE, out + (size_t)(128 * rt + 64) * 1280 + hg * 80, 1, lane, w);
    bar_lds();
    STAMP(8);
  }
  load_wo(smem, woB, hg, lane, w);
  if (w == 0) flag_wait(ws, WS_OF + (size_t)((16 + rt) * 8 + lane) * 128, lane < 8, tag);
  WAIT_VM(0);
  BAR();
  STAMP(9);
  outproj64(smem, orsrc, (128 + 8 * rt + 4 * br) * (int)O_TILE, out + (size_t)(128 * rt + 64 * br) * 1280 + 640 + hg * 80, 2, lane, w);
  STAMP(10);

  if (b == 0 && w == 0) {
#pragma unroll 1
    for (int k = 0; k < 4; ++k) flag_wait(ws, WS_KVF + (size_t)(k * 64 + lane) * 128, true, tag);
    if (lane == 0) flag_set(ws, WS_EPOCH, tag);
  }
}

extern "C" void kernel_launch(void* const* d_in, const int* in_sizes, int n_in,
                              void* d_out, int out_size, void* d_ws, size_t ws_size,
                              hipStream_t stream) {
  fused_kernel<<<NWG, NTHR, 0, stream>>>((const char*)d_in[0], (const char*)d_in[1], (const char*)d_in[2],
                                         (const char*)d_in[3], (const char*)d_in[4], (const char*)d_in[5],
                                         (__hip_bfloat16*)d_out, (char*)d_ws);
}
```

```cpp
#include <hip/hip_runtime.h>
#include <hip/hip_bf16.h>
#include <stdint.h>

typedef __bf16 bf16x8 __attribute__((ext_vector_type(8)));
typedef __bf16 bf16x2 __attribute__((ext_vector_type(2)));
typedef short s16x4 __attribute__((ext_vector_type(4)));
typedef float f32x4 __attribute__((ext_vector_type(4)));
typedef float f32x16 __attribute__((ext_vector_type(16)));
typedef unsigned u32x4 __attribute__((ext_vector_type(4)));
typedef unsigned u32x2 __attribute__((ext_vector_type(2)));
typedef unsigned long long u64;
#define LAS __attribute__((address_space(3)))
#define GAS __attribute__((address_space(1)))
#define DI __device__ __forceinline__

constexpr int NWG = 256, NTHR = 512;
constexpr int LDS_BYTES = 163840;
constexpr int WOFF = 16384, WBLK = 4480, WPC = 1088;
constexpr int STAGE = WOFF + 8 * WBLK;
constexpr int FLG = LDS_BYTES - 3072;
constexpr int PFS = FLG + 2048;
constexpr int BLK = 5376;
constexpr int KB = STAGE - 8 * BLK, VB = KB + 12 * BLK, QB = KB + 24 * BLK;
constexpr int RED = 640 * 160;
static_assert(KB >= 0 && KB % 256 == 0 && 3 * STAGE <= FLG && QB + 4 * 5120 <= FLG && RED + 40960 <= FLG && PFS + 1024 <= LDS_BYTES, "lds");
constexpr size_t WS_KVF = 4096;
constexpr size_t WS_OF = WS_KVF + 256 * 4 * 128;
constexpr size_t WS_KV = 1 << 20;
constexpr int KV_BYTES = 40960;
constexpr size_t WS_O = WS_KV + (size_t)256 * KV_BYTES;
constexpr int O_BLK = 40960;
constexpr int FIXN = 1;
constexpr int SPIN_CAP = 1 << 18;
constexpr int CODE_PART = 23, CODE_B = 168, CODE_A = 177;
#define KVF(wg, k) ((unsigned)WS_KVF + ((wg) * 4 + (k)) * 128)
#define OFL(wg, k) ((unsigned)WS_OF + ((wg) * 8 + (k)) * 128)

template <int V> struct IntC { static constexpr int value = V; };
DI f32x4 mfma16(bf16x8 a, bf16x8 b, f32x4 c) { return __builtin_amdgcn_mfma_f32_16x16x32_bf16(a, b, c, 0, 0, 0); }
DI f32x16 mfma32(u32x4 a, u32x4 b, f32x16 c) { return __builtin_amdgcn_mfma_f32_32x32x16_bf16(__builtin_bit_cast(bf16x8, a), __builtin_bit_cast(bf16x8, b), c, 0, 0, 0); }
DI f32x16 mfma32h(u32x2 a, u32x2 b, f32x16 c) { return __builtin_amdgcn_mfma_f32_32x32x8bf16_1k(__builtin_bit_cast(s16x4, a), __builtin_bit_cast(s16x4, b), c, 0, 0, 0); }
DI unsigned pk2(float a, float b) { bf16x2 v = {(__bf16)a, (__bf16)b}; return __builtin_bit_cast(unsigned, v); }
DI u32x2 pk4(f32x4 v) { u32x2 r = {pk2(v[0], v[1]), pk2(v[2], v[3])}; return r; }
DI u32x4 cat4(u32x2 lo, u32x2 hi) { u32x4 v = {lo[0], lo[1], hi[0], hi[1]}; return v; }
DI bf16x8 cat(u32x2 lo, u32x2 hi) { return __builtin_bit_cast(bf16x8, cat4(lo, hi)); }
DI u32x2 lds_r64(LAS char* p) { return *(volatile LAS u32x2*)p; }
DI u32x2 lds_tr(LAS char* p) { return __builtin_bit_cast(u32x2, __builtin_amdgcn_ds_read_tr16_b64_v4i16((LAS s16x4*)p)); }
template <int STEP>
DI void glds4(const char* sbase, const unsigned (&v)[4], unsigned lds) {
  unsigned keep;
  asm volatile("s_mov_b32 %0, m0\n\ts_mov_b32 m0, %6\n\ts_nop 0\n\tglobal_load_lds_dwordx4 %1, %5\n\t"
               "global_load_lds_dwordx4 %2, %5 offset:%c7\n\tglobal_load_lds_dwordx4 %3, %5 offset:%c8\n\t"
               "global_load_lds_dwordx4 %4, %5 offset:%c9\n\ts_mov_b32 m0, %0"
               : "=&s"(keep) : "v"(v[0]), "v"(v[1]), "v"(v[2]), "v"(v[3]), "s"(sbase), "s"(lds), "i"(STEP), "i"(2 * STEP), "i"(3 * STEP) : "memory");
}
DI void gtouch(const char* p, unsigned lds) {
  unsigned keep;
  asm volatile("s_mov_b32 %0, m0\n\ts_mov_b32 m0, %2\n\ts_nop 0\n\tglobal_load_lds_dword %1, off\n\ts_mov_b32 m0, %0"
               : "=&s"(keep) : "v"(p), "s"(lds) : "memory");
}
DI void st_wt(u32x4 v, __amdgpu_buffer_rsrc_t rsrc, int off) { __builtin_amdgcn_raw_buffer_store_b128(v, rsrc, off, 0, 16); }
DI u64 mix64(u64 x) { x += 0x9E3779B97F4A7C15ull; x = (x ^ (x >> 30)) * 0xBF58476D1CE4E5B9ull; x = (x ^ (x >> 27)) * 0x94D049BB133111EBull; return x ^ (x >> 31); }
#define SB() __builtin_amdgcn_sched_barrier(0)
#define WAIT_VM(n) asm volatile("s_waitcnt vmcnt(" #n ")" ::: "memory")
#define WAIT_LGKM0() do { asm volatile("" ::: "memory"); __builtin_amdgcn_s_waitcnt(0xC07F); asm volatile("" ::: "memory"); } while (0)
#define BAR() __builtin_amdgcn_s_barrier()
DI void bar_lds() { WAIT_LGKM0(); BAR(); asm volatile("" ::: "memory"); }
template <int NAP = 1>
DI void lds_wait_ge(LAS char* word, int target) {
#pragma unroll 1
  for (int it = 0; it < SPIN_CAP; ++it) { if (__all(*(volatile LAS int*)word >= target)) break; __builtin_amdgcn_s_sleep(NAP); }
  asm volatile("" ::: "memory");
}
DI void flag_set(char* ws, size_t off, u64 tag) { __hip_atomic_store((GAS u64*)(ws + off), tag, __ATOMIC_RELAXED, __HIP_MEMORY_SCOPE_AGENT); }
DI void flag_wait(char* ws, unsigned off, u64 tag) {
#pragma unroll 1
  for (int it = 0; it < SPIN_CAP; ++it) {
    const u64 v = __hip_atomic_load((GAS u64*)(ws + off), __ATOMIC_RELAXED, __HIP_MEMORY_SCOPE_AGENT);
    if (__all(v == tag)) break;
    __builtin_amdgcn_s_sleep(1);
  }
  asm volatile("" ::: "memory");
}

template <int N, int STEP>
DI void wo_pieces(u32x4 (&dst)[N], const char* wo, int hg, int pc0, int lane) {
  const __amdgpu_buffer_rsrc_t rs = __builtin_amdgcn_make_buffer_rsrc((void*)wo, 0, 640 * 1280, 0x00020000);
  const int e = pc0 * 64 + lane, row = (e * 6554) >> 16;
  int ch = e - row * 10, off = row * 1280 + ch * 16;
  constexpr int DR = STEP * 64 / 10, DC = STEP * 64 % 10;
#pragma unroll
  for (int k = 0; k < N; ++k) {
    dst[k] = __builtin_amdgcn_raw_buffer_load_b128(rs, off, hg * 160, 0);
    ch += DC;
    off += DR * 1280 + DC * 16 + (ch >= 10 ? 1280 - 160 : 0);
    ch -= ch >= 10 ? 10 : 0;
    SB();
  }
}

template <int KS, class PRE_, class MID_, class POST_>
DI void outproj(LAS char* smem, __amdgpu_buffer_rsrc_t orsrc, int oblk, __amdgpu_buffer_rsrc_t outr, int ooff, int lane, int w, char* ws, u64 tag,
                PRE_ begin, MID_ loop_done, POST_ wo_free) {
  constexpr int NG = 8 / KS, NM = 16 / KS, NS = 20 / KS, NF = NS * 5;
  const int r = lane & 15, g = lane >> 4, q = (lane >> 2) & 3, p = lane & 3;
  const int kq = KS == 2 ? w >> 2 : w & 3, tg = KS == 2 ? (w + kq) & 3 : w >> 2, blk = KS == 2 ? tg : 2 - 2 * tg;
  u32x4 of[2][NS];
  const int so = (oblk + blk) * O_BLK;
  const int vm = g * 512 + r * 16, vt = g * 512 + (g & 1) * 256 + r * 8;
  const unsigned fl = KS == 2 ? OFL((oblk >> 2) * 8 + kq * 4 + ((lane >> 1) & 3), 2 * blk + (lane & 1))
                              : OFL((oblk >> 2) * 8 + kq * 2 + ((lane >> 2) & 1), 4 * tg + (lane & 3));
  auto loads = [&]() {
#pragma unroll
    for (int s = 0; s < NS; ++s)
#pragma unroll
      for (int n = 0; n < 2; ++n)
      {
        if (s < NM) {
          of[n][s] = __builtin_amdgcn_raw_buffer_load_b128(orsrc, vm + n * 256, so + (kq * NM + s) * 2048, 16);
        } else {
          const int st = so + 32768 + (kq * (NS - NM) + s - NM) * 2048;
          of[n][s] = cat4(__builtin_amdgcn_raw_buffer_load_b64(orsrc, vt + n * 128, st, 16), __builtin_amdgcn_raw_buffer_load_b64(orsrc, (vt ^ 256) + n * 128, st, 16));
        }
      }
    SB();
  };
  begin(fl, loads);
  LAS char* wb = smem + kq * (NM * 6400) + p * 8;
  LAS char* wm = wb + (20 * (g >> 1) + 4 * (g & 1) + q) * 160;
  LAS char* wt0 = wb + (40 * g + 16 + q) * 160 + (g & 1) * 3200;
  LAS char* wt1 = wb + (40 * g + 16 + q) * 160 + (~g & 1) * 3200;
  auto rd = [&](int f) {
    const int s = f / 5, i = f % 5;
    return s < NM ? cat(lds_tr(wm + s * 6400 + i * 32), lds_tr(wm + s * 6400 + 1280 + i * 32))
                  : cat(lds_tr(wt0 + (s - NM) * 25600 + i * 32), lds_tr(wt1 + (s - NM) * 25600 + i * 32));
  };
  constexpr int PRE = 6;
  bf16x8 wf[NF];
#pragma unroll
  for (int f = 0; f < PRE; ++f) { wf[f] = rd(f); SB(); }
  f32x4 acc[2][5];
#pragma unroll
  for (int n = 0; n < 2; ++n)
#pragma unroll
    for (int i = 0; i < 5; ++i) acc[n][i] = f32x4{0.f, 0.f, 0.f, 0.f};
#pragma unroll
  for (int f = 0; f < NF; ++f) {
    if (f + PRE < NF) wf[f + PRE] = rd(f + PRE);
#pragma unroll
    for (int n = 0; n < 2; ++n) acc[n][f % 5] = mfma16(wf[f], __builtin_bit_cast(bf16x8, of[n][f / 5]), acc[n][f % 5]);
    __builtin_amdgcn_sched_group_barrier(0x8, 1, 0); __builtin_amdgcn_sched_group_barrier(0x100, 1, 0);
    __builtin_amdgcn_sched_group_barrier(0x8, 1, 0); __builtin_amdgcn_sched_group_barrier(0x100, 1, 0);
    SB();
  }
  loop_done();
  auto own = [](int n, int i) { return KS == 2 ? (i == 4 ? n : (i >> 1)) : (i == 4 ? 1 + n : 2 * n + (i >> 1)); };
  const int sstride = KS == 2 ? 0 : (tg ? 25600 : 10240);
  LAS char* sb[KS];
#pragma unroll
  for (int k = 0; k < KS; ++k) sb[k] = smem + lane * 16 + (KS == 2 ? RED + tg * 10240 : (tg ? 0 : RED)) + ((kq + k) & (KS - 1)) * sstride;
  if (KS == 4) {
    if (tg) lds_wait_ge(smem + FLG + 960 + kq * 4, 1);
    else *(volatile LAS int*)(smem + FLG + 960 + kq * 4) = 1;
  }
#pragma unroll
  for (int n = 0; n < 2; ++n)
#pragma unroll
    for (int i = 0; i < 5; ++i)
      if (own(n, i) != kq) *(LAS f32x4*)(sb[0] + (n * 5 + i) * 1024) = acc[n][i];
  if (KS == 2) {
    bar_lds();
  } else {
    asm volatile("" ::: "memory");
    *(volatile LAS int*)(smem + FLG + 976 + w * 4) = 1;
    lds_wait_ge<1>(smem + FLG + 976 + tg * 16 + (lane & 3) * 4, 1);
  }
  const int o = ooff + (blk * 32 + r) * 2560;
  auto sum = [&](int n, int i) {
    f32x4 v = acc[n][i];
#pragma unroll
    for (int k = 1; k < KS; ++k) v += *(LAS f32x4*)(sb[k] + (n * 5 + i) * 1024);
    return pk4(v);
  };
#pragma unroll
  for (int n = 0; n < 2; ++n) {
#pragma unroll
    for (int i = 0; i < 4; i += 2)
      if (own(n, i) == kq) {
        const u32x2 u0 = sum(n, i), u1 = sum(n, i + 1);
        auto x = __builtin_amdgcn_permlane16_swap(u0[0], u1[0], false, false);
        auto y = __builtin_amdgcn_permlane16_swap(u0[1], u1[1], false, false);
        __builtin_amdgcn_raw_buffer_store_b128(u32x4{x[0], y[0], x[1], y[1]}, outr, o + n * (16 * 2560) + i * 32 + (g & 1) * 32 + (g >> 1) * 16, 0, 0);
      }
    if (own(n, 4) == kq) __builtin_amdgcn_raw_buffer_store_b64(sum(n, 4), outr, o + n * (16 * 2560) + 128 + g * 8, 0, 0);
  }
  wo_free();
}

struct Loader { const char* xsrc; const char* wsrc; unsigned xoff[4], woff[4]; int rot; };
DI void quarter(const Loader& L, unsigned lds0, int s, int buf, int k) {
  const int sx = s + L.rot, sr = s < FIXN ? s : (sx >= 10 ? sx - (10 - FIXN) : sx);
  const unsigned d = lds0 + buf * STAGE;
  glds4<1024>(L.xsrc + sr * 128 + k * (32 * 1280), L.xoff, d + k * 4096);
  glds4<WPC>(L.wsrc + (size_t)(sr * 64 + k * 16) * 3840, L.woff, d + WOFF + 2 * k * WBLK);
  glds4<WPC>(L.wsrc + (size_t)(sr * 64 + k * 16 + 8) * 3840, L.woff, d + WOFF + (2 * k + 1) * WBLK);
}

template <int br>
DI void run_branch(LAS char* smem, const Loader& L, const char* __restrict__ woA, const char* __restrict__ woB,
                   __hip_bfloat16* __restrict__ out, char* ws) {
  const int tid = threadIdx.x, lane = tid & 63;
  const int w = __builtin_amdgcn_readfirstlane(tid >> 6);
  const int b = blockIdx.x, xcd = b & 7, idx = b >> 3;
  const int rt = ((xcd >> 1) & 1) * 8 + (idx & 7), hg = (xcd >> 2) * 4 + (idx >> 3);
  const int wgid = (br * 16 + rt) * 8 + hg;
  const int r = lane & 15, g = lane >> 4, q = (lane >> 2) & 3, p = lane & 3;

  const unsigned lds0 = (unsigned)(size_t)smem;
  const __amdgpu_buffer_rsrc_t kvrsrc = __builtin_amdgcn_make_buffer_rsrc(ws + WS_KV, 0, 256 * KV_BYTES, 0x00020000);
  const __amdgpu_buffer_rsrc_t orsrc = __builtin_amdgcn_make_buffer_rsrc(ws + WS_O, 0, 128 * O_BLK, 0x00020000);
  u64 epoch;
  u32x4 wor[25];
  if (w >= 4) {
    const int l = w - 4;
    auto quarter = [&](int s, int buf, int k) { ::quarter(L, lds0, s, buf, k); };
    auto wo_warm = [&](const char* wo) {
#pragma unroll
      for (int k = 0; k < 2; ++k) {
        const int sub = k * 64 + lane, id = idx * 100 + (sub < 100 ? sub : 99), row = (id * 13108) >> 16;
        gtouch(wo + (size_t)row * 1280 + (hg & 4) * 160 + (id - row * 5) * 128, lds0 + PFS);
      }
    };
    if (!br && l == 3) wo_warm(woA);
    bar_lds();
    quarter(1, 1, l);
    WAIT_VM(12); *(volatile LAS int*)(smem + FLG + l * 4) = 1;
    if (l < 2) {
      WAIT_VM(0); *(volatile LAS int*)(smem + FLG + 256 + l * 4) = 2;
    } else {
      quarter(2, 2, 2 * (l & 1));
      WAIT_VM(12); *(volatile LAS int*)(smem + FLG + 256 + l * 4) = 2;
      quarter(2, 2, 2 * (l & 1) + 1);
      WAIT_VM(0); *(volatile LAS int*)(smem + FLG + 512 + (l & 1) * 8 + (lane & 1) * 4) = 3;
    }
    if (l < 3) {
#pragma unroll 1
      for (int s = l + 3; s < 10; s += 3) {
        lds_wait_ge<1>(smem + FLG + 1024 + (lane & 3) * 256, s - 2);
#pragma unroll 1
        for (int k = 0; k < 4; ++k) {
          quarter(s, l, k);
        }
        if (s == 9) *(volatile LAS int*)(smem + FLG + 832) = 1;
        WAIT_VM(0);
        *(volatile LAS int*)(smem + FLG + l * 256 + (lane & 3) * 4) = s + 1;
      }
    } else {
      lds_wait_ge<8>(smem + FLG + 832, 1);
      wo_warm(woB);
    }
    if (!br) {
      lds_wait_ge<4>(smem + FLG + 832, 1);
      wo_pieces<25, 1>(wor, woA, hg, l * 25, lane);
    }
    asm volatile("s_load_dwordx2 %0, %1, 0x0\n\ts_waitcnt lgkmcnt(0)" : "=s"(epoch) : "s"(ws) : "memory");
    lds_wait_ge<4>(smem + FLG + 1024 + (lane & 3) * 256, 9);
    if (tid - 256 < 32) *(LAS u32x4*)(smem + KB + (((tid - 256) >> 1) + 8) * BLK + 5120 + (tid & 1) * 16) = u32x4{0x3F803F80u, 0x3F803F80u, 0x3F803F80u, 0x3F803F80u};
    bar_lds();
  } else {
    bar_lds();
    asm volatile("s_load_dwordx2 %0, %1, 0x0\n\ts_waitcnt lgkmcnt(0)" : "=s"(epoch) : "s"(ws) : "memory");
    auto gemm = [&](auto wc_) {
      constexpr int wc = decltype(wc_)::value, NT = 8 - wc, NST = 2 * NT, LA = 4;
      const int wt = w & 1;
      int xa[2], wa[2], pos[5];
#pragma unroll
      for (int ks = 0; ks < 2; ++ks) xa[ks] = (64 * wt + r) * 128 + (((4 * ks + g) ^ ((r >> 1) & 7)) << 4);
      wa[0] = WOFF + g * WBLK + (q >> 1) * WPC + (q & 1) * (512 + 32) + p * 8;
      wa[1] = WOFF + g * WBLK + (q >> 1) * WPC + (q & 1) * (512 - 32) + p * 8;
#pragma unroll
      for (int t = 0; t < 5; ++t) pos[t] = (g * 16 + (wc ? r ^ (4 * (((4 * t + g) % 5) & 3)) : r)) * 16 + t * 1024;
      f32x4 acc[NT][4];
#pragma unroll
      for (int i = 0; i < NT; ++i)
#pragma unroll
        for (int j = 0; j < 4; ++j) acc[i][j] = f32x4{0.f, 0.f, 0.f, 0.f};
      bf16x8 wf[LA], xf[2][4], wv[NST + LA];
      auto rw = [&](LAS char* base, int st) {
        const int ks = st / NT, t = st % NT, c = t < 5 ? 5 + 5 * wc + t : 3 * wc + t - 5;
        LAS char* a_ = base + wa[c & 1] + c * 32 + ks * (4 * WBLK);
        return cat(lds_tr(a_), lds_tr(a_ + 2 * WPC));
      };
      auto rx = [&](LAS char* base, int ks, int j) { xf[ks][j] = *(LAS bf16x8*)(base + xa[ks] + j * 2048); };
      auto mma = [&](int st) {
#pragma unroll
        for (int j = 0; j < 4; ++j) acc[st % NT][j] = mfma16(wv[st], xf[st / NT][j], acc[st % NT][j]);
#pragma unroll
        for (int j = 0; j < 4; ++j) { __builtin_amdgcn_sched_group_barrier(0x8, 1, 0); __builtin_amdgcn_sched_group_barrier(0x100, 1, 0); }
        SB();
      };
      volatile LAS int* fre = (volatile LAS int*)(smem + FLG + 1024 + w * 256 + lane * 4);
      LAS char* fullp = smem + FLG + (lane & 3) * 4;
#pragma unroll
      for (int i = 0; i < NT; ++i) asm volatile("" : "+v"(acc[i][0]), "+v"(acc[i][1]), "+v"(acc[i][2]), "+v"(acc[i][3]));
      asm volatile("" : "+v"(xa[0]), "+v"(xa[1]), "+v"(wa[0]), "+v"(wa[1]), "+v"(pos[0]), "+v"(pos[1]), "+v"(pos[2]), "+v"(pos[3]), "+v"(pos[4]));
      __builtin_amdgcn_s_setprio(3);
      lds_wait_ge(fullp, 1);
      wf[0] = rw(smem, 0);
#pragma unroll
      for (int j = 0; j < 4; ++j) rx(smem, 0, j);
#pragma unroll
      for (int st = 1; st < LA; ++st) wf[st] = rw(smem, st);
      int buf = 0, s = 0;
#pragma unroll 1
      do {
        const int nb_ = buf == 2 ? 0 : buf + 1;
        LAS char* base = smem + buf * STAGE;
        LAS char* nbase = smem + nb_ * STAGE;
        int fl;
#pragma unroll
        for (int st = 0; st < LA; ++st) wv[st] = wf[st];
        SB();
#pragma unroll
        for (int st = 0; st < NST; ++st) {
          if (st == NST - LA) {
            if (__builtin_expect(!__all(fl >= s + 2), 0)) lds_wait_ge(fullp + nb_ * 256, s + 2);
            SB();
          }
          wv[st + LA] = st + LA < NST ? rw(base, st + LA) : rw(nbase, st + LA - NST);
          if (st >= 1 && st <= 4) rx(base, 1, st - 1);
          if (st == NST - LA - 2) fl = *(volatile LAS int*)(fullp + nb_ * 256);
          if (st == NST - LA - 1) { asm volatile("" ::: "memory"); *fre = s + 1; }
          if (st == NST - LA || st == NST - LA + 1) { rx(nbase, 0, 2 * (st - NST + LA)); rx(nbase, 0, 2 * (st - NST + LA) + 1); }
          mma(st);
        }
#pragma unroll
        for (int st = 0; st < LA; ++st) wf[st] = wv[NST + st];
        buf = nb_;
      } while (++s < 9);
      const float CS = 0.22360679774997896f * 1.4426950408889634f;
      const int kvo = (br || wt) ? wgid * KV_BYTES + wc * 20480 + 2 * wt * 5120 : 0x7FFF0000;
      LAS char* kvl = smem + (wc ? VB : KB) + (8 + 2 * wt) * BLK;
      LAS char* ql = smem + QB + 2 * wt * 5120 + 3 * wc * 1024 + lane * 16;
      auto emit = [&](int t, int jb) {
        if (t < 5) {
          const u32x4 v = cat4(pk4(acc[t][2 * jb]), pk4(acc[t][2 * jb + 1]));
          if (br || jb == 1) st_wt(v, kvrsrc, kvo + jb * 5120 + pos[t]);
          *(LAS u32x4*)(kvl + jb * BLK + pos[t]) = v;
        } else {
          *(LAS u32x4*)(ql + jb * 5120 + (t - 5) * 1024) = cat4(pk4(acc[t][2 * jb] * CS), pk4(acc[t][2 * jb + 1] * CS));
        }
      };
      int fl;
#pragma unroll
      for (int st = 0; st < LA; ++st) wv[st] = wf[st];
      SB();
#pragma unroll
      for (int st = 0; st < NST; ++st) {
        if (st == NT) {
          if (__builtin_expect(!__all(fl >= 9), 0)) lds_wait_ge(smem + FLG + 1024 + (lane & 3) * 256, 9);
          SB();
        }
        if (st + LA < NST) wv[st + LA] = rw(smem, st + LA);
        if (st >= 1 && st <= 4) rx(smem, 1, st - 1);
        if (st == NT - 2) fl = *(volatile LAS int*)(smem + FLG + 1024 + (lane & 3) * 256);
        if (st >= NT + 1) emit(st - NT - 1, 0);
        if (st >= NT + 2) emit(st - NT - 2, 1);
        mma(st);
      }
      emit(NT - 2, 1); emit(NT - 1, 0); emit(NT - 1, 1);
      __builtin_amdgcn_s_setprio(0);
    };
    if (w < 2) gemm(IntC<0>{}); else gemm(IntC<1>{});
    bar_lds();
    WAIT_VM(0);
    if (lane == 0) flag_set(ws, KVF(wgid, w), mix64(epoch));
  }
  const u64 tag = mix64(epoch);

  {
    const int qb = br ? (w < 4 ? 3 : 1) : w >> 1, h0 = br ? w & 3 : 2 * (w & 1);
    const int key = lane & 31, h2 = lane >> 5, bs = g & 1;
    u32x4 wob[25];
    const int nd = w < 4 ? 0 : min(rt, 2);
    u32x4 (&nb)[25] = wob;
    const unsigned nbf = nd > 0 ? KVF(wgid - 8 * (((lane >> 2) & 1) < nd ? ((lane >> 2) & 1) + 1 : 1), lane & 3) : KVF(wgid, 0);
    auto nb_issue = [&]() {
      const int l = w - 4;
      const int so = (wgid - 8) * KV_BYTES + l * 10240;
#pragma unroll
      for (int k = 0; k < 20; ++k) {
        const bool on = k < 10 || nd > 1;
        nb[k] = __builtin_amdgcn_raw_buffer_load_b128(kvrsrc, on ? lane * 16 : 0x7FFF0000, k < 10 ? so + k * 1024 : (on ? so - 8 * KV_BYTES + (k - 10) * 1024 : 0), 16);
      }
      SB();
      *(volatile LAS int*)(smem + FLG + 784 + l * 4) = 1;
    };
    auto nb_commit = [&]() {
      const int l = w - 4;
      LAS char* dst = smem + ((l >> 1) ? VB : KB) + (4 + 2 * (l & 1)) * BLK + lane * 16;
      if (lane < 2) *(LAS u32x4*)(smem + KB + (2 * l + lane) * BLK + 5120) = u32x4{0x3F803F80u, 0x3F803F80u, 0x3F803F80u, 0x3F803F80u};
#pragma unroll
      for (int k = 0; k < 10; ++k) *(LAS u32x4*)(dst + (k / 5) * BLK + (k % 5) * 1024) = nb[k];
      asm volatile("" ::: "memory");
      *(volatile LAS int*)(smem + FLG + 768 + l * 4) = 1;
#pragma unroll
      for (int k = 10; k < 20; ++k) *(LAS u32x4*)(dst - 4 * BLK + ((k - 10) / 5) * BLK + (k % 5) * 1024) = nb[k];
      asm volatile("" ::: "memory");
      *(volatile LAS int*)(smem + FLG + 768 + l * 4) = 2;
    };
    auto get_wo = [&]() {
      if (rt > 0) lds_wait_ge(smem + FLG + 784 + (lane & 3) * 4, 1);
      wo_pieces<25, 1>(wob, woB, hg, w * 25, lane);
    };
    if (w >= 4 && br) __builtin_amdgcn_s_setprio(2);
    const float NEG = -__builtin_inff();
    const u64 xa_ = (u64)0x5555u << (key + 18);
    const unsigned inv_d = (br ? ~((2u << key) - 1u) : ~(unsigned)(xa_ >> 32)) >> (4 * h2);
    const unsigned inv_f = (br ? (2u << key) - 1u : ~(unsigned)xa_) >> (4 * h2);
    auto mask = [&](int kind) {
      int bits = kind == 0 ? 0 : (kind == 1 ? inv_d : (kind == 2 ? inv_f : -1));
      asm volatile("" : "+v"(bits));
      f32x16 m;
#pragma unroll
      for (int i = 0; i < 16; ++i) m[i] = __uint_as_float((unsigned)__builtin_amdgcn_sbfe(bits, (i & 3) + 8 * (i >> 2), 1) & 0xFF800000u);
      return m;
    };
    auto maskin = [&](f32x16& x, unsigned bits) {
#pragma unroll
      for (int i = 0; i < 16; ++i) x[i] = (bits >> ((i & 3) + 8 * (i >> 2))) & 1 ? NEG : x[i];
    };
    const int kpos = (key & 15) * 16 + (key >> 4) * 8;
    const int k1o = (5 * h0 + 2 * h2) * 256 + kpos;
    const int k2o = h2 ? 5120 : (5 * h0 + 4) * 256 + kpos, k2s = h2 ? 0 : 1280;
    int vo[2];
#pragma unroll
    for (int t = 0; t < 2; ++t) vo[t] = (bs && p) ? 5128 : (5 * h0 + (bs ? 4 : p)) * 256 + ((8 * t + 4 * h2 + q) ^ (bs ? 0 : 4 * p)) * 16;
    const int vs = (bs && p) ? 0 : 1280;
    u32x4 q1[2]; u32x2 q2[2];
    f32x16 o[2], s[2];
    u32x4 k1[2]; u32x2 k2[2];
    auto ldkh = [&](int off, int h) {
      LAS char* kb = smem + KB + off;
      k1[h] = cat4(lds_r64(kb + k1o + h * 1280), lds_r64(kb + k1o + h * 1280 + 256)); k2[h] = lds_r64(kb + k2o + h * k2s);
    };
    auto ldk = [&](int beta) { ldkh(beta * BLK, 0); ldkh(beta * BLK, 1); };
    auto qk = [&](int h, const f32x16& c) { s[h] = mfma32h(k2[h], q2[h], mfma32(k1[h], q1[h], c)); };
    auto pexp = [&](int h, int st) {
      u32x4 pf;
#pragma unroll
      for (int j = 0; j < 4; ++j) pf[j] = pk2(__builtin_amdgcn_exp2f(s[h][8 * st + 2 * j]), __builtin_amdgcn_exp2f(s[h][8 * st + 2 * j + 1]));
      return pf;
    };
    auto ldv = [&](int off, int h, u32x4 (&vf)[2]) {
      LAS char* vb = smem + VB + off + h * vs;
#pragma unroll
      for (int st = 0; st < 2; ++st) vf[st] = cat4(lds_tr(vb + vo[0] + st * 8), lds_tr(vb + vo[1] + st * 8));
    };
    auto setm = [&](int h, float m) {
      const float a = -m, a0 = __uint_as_float(__float_as_uint(a) & 0xFFFF0000u), r1 = a - a0, a1 = __uint_as_float(__float_as_uint(r1) & 0xFFFF0000u), r2 = r1 - a1;
      q2[h][0] = h2 ? (__float_as_uint(a0) >> 16) | __float_as_uint(a1) : q2[h][0];
      q2[h][1] = h2 ? __float_as_uint(r2) >> 16 : q2[h][1];
    };
    auto max3 = [](float x, float y, float z) { return fmaxf(fmaxf(x, y), z); };
    auto rowmax = [&](const f32x16& x, float m) {
      return max3(max3(max3(x[0], x[1], x[2]), max3(x[3], x[4], x[5]), max3(x[6], x[7], x[8])), max3(max3(x[9], x[10], x[11]), max3(x[12], x[13], x[14]), x[15]), m);
    };
    auto xhalf = [&](float m) {
      auto a = __builtin_amdgcn_permlane32_swap(__float_as_uint(m), __float_as_uint(m), false, false);
      return fmaxf(__uint_as_float(a[0]), __uint_as_float(a[1]));
    };
    const int bd = 8 + qb;
    const int bf = br ? qb : bd - 1;
    const int lo = __builtin_amdgcn_readfirstlane(max(bf, 8 - 4 * rt));
    const f32x16 zero16 = {0.f, 0.f, 0.f, 0.f, 0.f, 0.f, 0.f, 0.f, 0.f, 0.f, 0.f, 0.f, 0.f, 0.f, 0.f, 0.f};
#pragma unroll
    for (int u = 0; u < 2; ++u) {
      LAS char* qp = smem + QB + qb * 5120 + (br ? -5120 * u : 1280 * u);
      q1[u] = cat4(lds_r64(qp + k1o), lds_r64(qp + k1o + 256));
      q2[u] = lds_r64(qp + (5 * h0 + 4) * 256 + kpos);
      q2[u][0] = h2 ? 0u : q2[u][0]; q2[u][1] = h2 ? 0u : q2[u][1];
    }
    auto publish = [&]() {
      *(volatile LAS int*)(smem + FLG + 800 + w * 4) = 1;
      u32x2 tl[2];
      const int so = (br * 64 + 4 * rt + qb) * O_BLK;
#pragma unroll
      for (int u = 0; u < 2; ++u) {
        const float inv = __builtin_amdgcn_rcpf(o[u][12]);
        const u32x4 mv = {pk2(o[u][0] * inv, o[u][1] * inv), pk2(o[u][2] * inv, o[u][3] * inv), pk2(o[u][4] * inv, o[u][5] * inv), pk2(o[u][6] * inv, o[u][7] * inv)};
        tl[u] = u32x2{pk2(o[u][8] * inv, o[u][9] * inv), pk2(o[u][10] * inv, o[u][11] * inv)};
        st_wt(mv, orsrc, so + (4 * hg + h0) * 1024 + (br ? -O_BLK * u : 1024 * u) + lane * 16);
      }
      {
        auto x = __builtin_amdgcn_permlane32_swap(tl[0][0], tl[1][0], false, false);
        auto y = __builtin_amdgcn_permlane32_swap(tl[0][1], tl[1][1], false, false);
        __builtin_amdgcn_raw_buffer_store_b64(u32x2{x[0], y[0]}, orsrc, so + 32768 + (4 * hg + h0) * 256 + h2 * (br ? -O_BLK : 256) + key * 8, 0, 16);
      }
      if (!br) *(volatile LAS int*)(smem + FLG + 800 + w * 4) = 2;
      WAIT_VM(0);
      if (lane == 0) flag_set(ws, OFL(wgid, w), tag);
    };
    auto put_wo = [&](const u32x4 (&src)[25]) {
      lds_wait_ge(smem + FLG + 800 + (lane & 7) * 4, 1);
#pragma unroll
      for (int k = 0; k < 25; ++k) *(LAS u32x4*)(smem + ((w & 3) * 25 + k) * 1024 + lane * 16) = src[k];
    };
    const __amdgpu_buffer_rsrc_t outr = __builtin_amdgcn_make_buffer_rsrc(out, 0, 2048 * 2560, 0x00020000);
    if constexpr (!br) {
      const int offd = bd * BLK, offp = (qb == 0 && rt == 0) ? offd : offd - BLK;
      float md[2];
      u32x4 vd[2][2];
      {
        const f32x16 cmd = mask(1);
        ldk(bd); ldv(offd, 0, vd[0]); ldv(offd, 1, vd[1]);
        qk(0, cmd); qk(1, cmd);
        md[0] = rowmax(s[0], NEG); md[1] = rowmax(s[1], NEG);
      }
      const f32x16 cmp_ = mask((qb == 0 && rt == 0) ? 3 : 2);
      asm volatile("" :: "v"(md[0]), "v"(md[1]), "v"(cmp_));
      SB();
      if (rt > 0 && qb == 0) {
        flag_wait(ws, KVF(wgid - 8, 1 + 2 * (w & 1)), tag);
        u32x4 hb[5];
#pragma unroll
        for (int k = 0; k < 5; ++k) hb[k] = __builtin_amdgcn_raw_buffer_load_b128(kvrsrc, lane * 16, (wgid - 8) * KV_BYTES + (w & 1) * 20480 + 15360 + k * 1024, 16);
        if (lane == 0) *(LAS u32x4*)(smem + KB + 7 * BLK + 5120) = u32x4{0x3F803F80u, 0x3F803F80u, 0x3F803F80u, 0x3F803F80u};
#pragma unroll
        for (int k = 0; k < 5; ++k) *(LAS u32x4*)(smem + ((w & 1) ? VB : KB) + 7 * BLK + lane * 16 + k * 1024) = hb[k];
        asm volatile("" ::: "memory");
        *(volatile LAS int*)(smem + FLG + 768 + (w & 1) * 4) = 1;
        lds_wait_ge(smem + FLG + 768 + (lane & 1) * 4, 1);
      }
      ldkh(offp, 0); ldkh(offp, 1);
      u32x4 vp[2];
      f32x16 sp[2];
#pragma unroll
      for (int h = 0; h < 2; ++h) {
        LAS char* vb = smem + VB + offp + h * vs;
        vp[h] = cat4(lds_tr(vb + vo[0] + 8), lds_tr(vb + vo[1] + 8));
      }
      SB();
#pragma unroll
      for (int h = 0; h < 2; ++h) sp[h] = mfma32h(k2[h], q2[h], mfma32(k1[h], q1[h], cmp_));
#pragma unroll
      for (int h = 0; h < 2; ++h) {
        float m = fmaxf(fmaxf(fmaxf(md[h], sp[h][8]), fmaxf(sp[h][9], sp[h][10])), fmaxf(fmaxf(sp[h][11], sp[h][12]), fmaxf(fmaxf(sp[h][13], sp[h][14]), sp[h][15])));
        m = xhalf(m);
        s[h] -= m;
        o[h] = mfma32(vd[h][0], pexp(h, 0), zero16);
        o[h] = mfma32(vd[h][1], pexp(h, 1), o[h]);
#pragma unroll
        for (int i = 8; i < 16; ++i) s[h][i] = sp[h][i] - m;
        o[h] = mfma32(vp[h], pexp(h, 1), o[h]);
      }
      publish();
      if (w >= 4) {
        put_wo(wor);
        asm volatile("" ::: "memory");
        *(volatile LAS int*)(smem + FLG + 864 + (w - 4) * 4) = 1;
      }
      if (b == 0 && w == 1) {
#pragma unroll 1
        for (int it = 0; it < SPIN_CAP; ++it) {
          bool ok = true;
#pragma unroll
          for (int k = 0; k < 4; ++k) ok &= __hip_atomic_load((GAS u64*)(ws + KVF(k * 64 + lane, 0)), __ATOMIC_RELAXED, __HIP_MEMORY_SCOPE_AGENT) == tag;
          if (__all(ok)) break;
          __builtin_amdgcn_s_sleep(1);
        }
        if (lane == 0) flag_set(ws, 0, tag);
      }
      lds_wait_ge(smem + FLG + 800 + (lane & 7) * 4, 2);
      u32x4 wo2[13];
      outproj<2>(smem, orsrc, 4 * rt, outr, (128 * rt) * 2560 + hg * 160, lane, w, ws, tag,
                 [&](unsigned fl, auto loads) {
                   flag_wait(ws, fl, tag); loads();
                   *(volatile LAS int*)(smem + FLG + 896 + w * 4) = 1;
                   lds_wait_ge(smem + FLG + 864 + (lane & 3) * 4, 1);
                 },
                 [&]() {
                   lds_wait_ge(smem + FLG + 896 + (lane & 7) * 4, 1);
                   wo_pieces<13, 8>(wo2, woB, hg, w, lane);
                 },
                 [&]() {
#pragma unroll
                   for (int k = 0; k < 13; ++k)
                     if (k < 12 || w < 4) *(LAS u32x4*)(smem + (k * 8 + w) * 1024 + lane * 16) = wo2[k];
                   asm volatile("" ::: "memory");
                   *(volatile LAS int*)(smem + FLG + 928 + w * 4) = 1;
                 }
                );
      outproj<4>(smem, orsrc, 64 + 4 * rt, outr, (128 * rt) * 2560 + 1280 + hg * 160, lane, w, ws, tag,
                 [&](unsigned fl, auto loads) { flag_wait(ws, fl, tag); loads(); lds_wait_ge(smem + FLG + 928 + (lane & 7) * 4, 1); },
                 [&]() {}, [&]() {});
    } else {
    int off;
    u32x4 vf[2];
    const int lo1 = __builtin_amdgcn_readfirstlane(max(qb - 1, 8 - 4 * rt));
    auto ldk1 = [&](int o_) { k1[0] = cat4(lds_r64(smem + KB + o_ + k1o), lds_r64(smem + KB + o_ + k1o + 256)); };
    auto ldk2 = [&](int o_) { k2[0] = lds_r64(smem + KB + o_ + k2o); };
    auto ldv1 = [&](int o_, int st) { vf[st] = cat4(lds_tr(smem + VB + o_ + vo[0] + st * 8), lds_tr(smem + VB + o_ + vo[1] + st * 8)); };
    auto qku = [&](int u, const f32x16& c) { return mfma32h(k2[0], q2[u], mfma32(k1[0], q1[u], c)); };
#define SG(m, k) __builtin_amdgcn_sched_group_barrier(m, k, 0)
    auto body = [&](auto have1, int nx) {
      constexpr bool HAVE1 = decltype(have1)::value;
      SB();
      ldv1(off, 1);
      f32x16 t1;
      if (!HAVE1) t1 = mfma32(k1[0], q1[1], zero16);
      const u32x4 p0 = pexp(0, 0);
      SG(0x8, 1); SG(0x100, 2); SG(0x400, 8);
      SB();
      ldk1(nx);
      if (!HAVE1) s[1] = mfma32h(k2[0], q2[1], t1);
      const u32x4 p1 = pexp(0, 1);
      o[0] = mfma32(vf[0], p0, o[0]);
      if (!HAVE1) SG(0x8, 1);
      SG(0x100, 2); SG(0x400, 4); SG(0x8, 1); SG(0x400, 4);
      SB();
      ldk2(nx);
      o[0] = mfma32(vf[1], p1, o[0]);
      const u32x4 p2 = pexp(1, 0);
      const f32x16 t0 = mfma32(k1[0], q1[0], zero16);
      SG(0x8, 1); SG(0x100, 1); SG(0x400, 4); SG(0x8, 1); SG(0x400, 4);
      SB();
      o[1] = mfma32(vf[0], p2, o[1]);
      const u32x4 p3 = pexp(1, 1);
      s[0] = mfma32h(k2[0], q2[0], t0);
      SG(0x8, 1); SG(0x400, 4); SG(0x8, 1); SG(0x400, 4);
      SB();
      ldv1(nx, 0);
      o[1] = mfma32(vf[1], p3, o[1]);
      SG(0x100, 2); SG(0x8, 1);
      SB();
      off = nx;
    };
    off = (bd - 1) * BLK;
    {
      const f32x16 cm = mask(1);
      ldk1(off + BLK); ldk2(off + BLK); ldv1(off + BLK, 0); ldv1(off + BLK, 1);
      s[0] = qku(0, cm);
      ldk1(off); ldk2(off);
      s[1] = qku(1, cm);
      const float m0 = xhalf(rowmax(s[0], NEG)), m1 = xhalf(rowmax(s[1], NEG));
      setm(0, m0); setm(1, m1);
      s[0] -= m0; s[1] -= m1;
      asm volatile("" : "+v"(s[0]), "+v"(s[1]), "+v"(q2[0]), "+v"(q2[1]));
    }
    auto own_first = [&]() {
      o[0] = mfma32(vf[0], pexp(0, 0), zero16);
      o[0] = mfma32(vf[1], pexp(0, 1), o[0]);
      o[1] = zero16;
      ldv1(off, 0);
      s[0] = qku(0, zero16);
      body(IntC<1>{}, w < 4 ? off - BLK : off);
    };
    own_first();
    if (nd > 0) { flag_wait(ws, nbf, tag); nb_issue(); nb_commit(); }
    __builtin_amdgcn_s_setprio(0);
    if (w < 4) {
      body(IntC<0>{}, off - BLK);
      body(IntC<0>{}, off);
      get_wo();
    }
    if (lo1 < 8) {
      lds_wait_ge(smem + FLG + 768 + (lane & 3) * 4, 1);
      off = 7 * BLK;
      ldk1(off); ldk2(off); ldv1(off, 0);
      s[0] = qku(0, zero16);
#pragma unroll 1
      for (int beta = 7; beta >= lo; --beta) {
        if (__builtin_expect(beta == 4 && lo1 < 4, 0)) lds_wait_ge(smem + FLG + 768 + (lane & 3) * 4, 2);
        if (__builtin_expect(beta == qb, 0)) maskin(s[0], inv_f);
        body(IntC<0>{}, beta > lo1 ? off - BLK : off);
      }
      if (lo1 < lo) {
        ldv1(off, 1);
        s[1] = qku(1, mask(2));
        o[1] = mfma32(vf[0], pexp(1, 0), o[1]);
        o[1] = mfma32(vf[1], pexp(1, 1), o[1]);
      }
    }
    if (__builtin_expect(!__all(o[0][12] <= 0x1p40f && o[1][12] <= 0x1p40f), 0)) {
      float m0 = NEG, m1 = NEG;
      setm(0, 0.f); setm(1, 0.f);
#pragma unroll 1
      for (int pass = 0; pass < 2; ++pass) {
        o[0] = zero16; o[1] = zero16;
#pragma unroll 1
        for (int beta = bd; beta >= lo1; --beta) {
          ldk1(beta * BLK); ldk2(beta * BLK); ldv1(beta * BLK, 0); ldv1(beta * BLK, 1);
          s[0] = qku(0, mask(beta == bd ? 1 : (beta == qb ? 2 : (beta < qb ? 3 : 0))));
          asm volatile("" : "+v"(s[0]));
          SB();
          s[1] = qku(1, mask(beta == bd ? 3 : (beta == bd - 1 ? 1 : (beta == qb - 1 ? 2 : 0))));
          asm volatile("" : "+v"(s[1]));
          SB();
          if (pass) {
#pragma unroll
            for (int u = 0; u < 2; ++u) { o[u] = mfma32(vf[0], pexp(u, 0), o[u]); o[u] = mfma32(vf[1], pexp(u, 1), o[u]); }
          } else {
            m0 = rowmax(s[0], m0); m1 = rowmax(s[1], m1);
          }
        }
        if (!pass) { setm(0, xhalf(m0)); setm(1, xhalf(m1)); }
      }
      wo_pieces<25, 1>(wob, woB, hg, (w & 3) * 25, lane);
    }
    publish();
    outproj<4>(smem, orsrc, 64 + 4 * rt + 1, outr, (128 * rt + 32) * 2560 + 1280 + hg * 160, lane, w, ws, tag,
               [&](unsigned fl, auto loads) {
                 auto put = [&]() { put_wo(wob); asm volatile("" ::: "memory"); *(volatile LAS int*)(smem + FLG + 928 + w * 4) = 1; };
                 bool put_first = false;
                 if (w < 4) {
#pragma unroll 1
                   for (int it = 0; it < SPIN_CAP; ++it) {
                     if (__all(*(volatile LAS int*)(smem + FLG + 800 + (lane & 7) * 4) >= 1)) { put_first = true; break; }
                     if (__all(__hip_atomic_load((GAS u64*)(ws + fl), __ATOMIC_RELAXED, __HIP_MEMORY_SCOPE_AGENT) == tag)) break;
                     __builtin_amdgcn_s_sleep(1);
                   }
                   if (put_first) put();
                 }
                 if (w >= 4 || put_first) flag_wait(ws, fl, tag);
                 asm volatile("" ::: "memory");
                 loads();
                 if (w >= 4) lds_wait_ge(smem + FLG + 928 + (w - 4) * 4, 1);
                 else if (!put_first) put();
               },
               [&]() {}, [&]() {});
    }
  }
  __builtin_amdgcn_endpgm();
}

__global__ void __launch_bounds__(NTHR, 2)
fused_kernel(const char* __restrict__ xA, const char* __restrict__ wqA, const char* __restrict__ woA,
             const char* __restrict__ xB, const char* __restrict__ wqB, const char* __restrict__ woB,
             __hip_bfloat16* __restrict__ out, char* ws) {
  __shared__ __attribute__((aligned(16))) char smem_[LDS_BYTES];
  LAS char* smem = (LAS char*)smem_;
  const unsigned lds0 = (unsigned)(size_t)smem;
  const int tid = threadIdx.x, lane = tid & 63;
  const int w = __builtin_amdgcn_readfirstlane(tid >> 6);
  const int xcd = blockIdx.x & 7, idx = blockIdx.x >> 3, br = xcd & 1;
  Loader L;
  if (w >= 4) {
    L.xsrc = (br ? xB : xA) + (size_t)(((xcd >> 1) & 1) * 8 + (idx & 7)) * 128 * 1280;
    L.wsrc = (br ? wqB : wqA) + ((xcd >> 2) * 4 + (idx >> 3)) * 160;
    L.rot = (((idx & 7) + (idx >> 3) * 3)) % (10 - FIXN);
#pragma unroll
    for (int i = 0; i < 4; ++i) {
      const int row = 8 * i + (lane >> 3);
      L.xoff[i] = row * 1280 + (((lane & 7) ^ ((row >> 1) & 7)) << 4) - i * 1024;
      const int odd = lane >> 5, pc = lane & 31;
      int c = (pc >> 1) ^ odd;
      c = c == 15 ? 14 : c;
      const int seg = (c * 13) >> 6, c5 = c - 5 * seg;
      L.woff[i] = (2 * i + odd) * 3840 + seg * 1280 + c5 * 32 + (pc & 1) * 16 - i * WPC;
    }
    quarter(L, lds0, 0, 0, w - 4);
  } else {
    const char* pc;
    asm volatile("s_getpc_b64 %0" : "=s"(pc));
    const int n = br ? CODE_B : CODE_A, ln = (idx & 7) * CODE_PART + lane;
    if (w == 0 && lane < CODE_PART && ln < n) gtouch(pc + ((br ? 0 : CODE_B) + ln) * 128, lds0 + PFS);
  }
  *(LAS int*)(smem + FLG + tid * 4) = 0;
  if (br) run_branch<1>(smem, L, woA, woB, out, ws);
  else run_branch<0>(smem, L, woA, woB, out, ws);
}

extern "C" void kernel_launch(void* const* d_in, const int* in_sizes, int n_in,
                              void* d_out, int out_size, void* d_ws, size_t ws_size,
                              hipStream_t stream) {
  fused_kernel<<<NWG, NTHR, 0, stream>>>((const char*)d_in[0], (const char*)d_in[1], (const char*)d_in[2],
                                         (const char*)d_in[3], (const char*)d_in[4], (const char*)d_in[5],
                                         (__hip_bfloat16*)d_out, (char*)d_ws);
}
```
